```python
import torch

def _softmax(x, axis=-1):
    return torch.softmax(x, dim=axis)
def _gelu(x):
    return torch.nn.functional.gelu(x, approximate="tanh")
def _silu(x):
    return torch.nn.functional.silu(x)
def _sigmoid(x):
    return torch.sigmoid(x)
def _layernorm(x, axis=-1, eps=1e-5):
    m = x.mean(dim=axis, keepdim=True)
    v = x.var(dim=axis, keepdim=True, unbiased=False)
    return (x - m) / torch.sqrt(v + eps)
def _scatter_add(tgt, idx, src, axis=0):
    return tgt.clone().index_add_(axis, idx, src)
def _segment_sum(src, seg, n):
    out = torch.zeros((n,) + src.shape[1:], dtype=src.dtype)
    return out.index_add_(0, seg, src)
def _pad(x, pw):
    flat = tuple(v for pair in reversed(pw) for v in pair)
    return torch.nn.functional.pad(x, flat)

kernel_name = 'recurrent_scan__sliding_local_dilated__swiglu_ffn__prompt'

def setup_inputs():

    _w = (lambda a: (lambda t: t.to(torch.bfloat16) if t.is_floating_point() else t)(torch.as_tensor(a)))
    def _A_setup():
        B, S, D = 1, 2048, 448
        return {
            "x": _w((lambda *s: torch.randn(*s, dtype=torch.bfloat16) * 0.05)(B, S, D)),
            "wqkv": _w((lambda *s: torch.randn(*s, dtype=torch.bfloat16) * 0.05)(D, 3 * D)),
            "wo": _w((lambda *s: torch.randn(*s, dtype=torch.bfloat16) * 0.05)(D, D)),
        }
    _A_in = _A_setup()

    def _B_setup():
        B, S, D, F = 1, 2048, 448, 1792
        return {
            "x": _w((lambda *s: torch.randn(*s, dtype=torch.bfloat16) * 0.05)(B, S, D)),
            "w_gate": _w((lambda *s: torch.randn(*s, dtype=torch.bfloat16) * 0.05)(D, F)),
            "w_up": _w((lambda *s: torch.randn(*s, dtype=torch.bfloat16) * 0.05)(D, F)),
            "w_down": _w((lambda *s: torch.randn(*s, dtype=torch.bfloat16) * 0.05)(F, D)),
        }
    _B_in = _B_setup()

    return {
        "A_x": _A_in["x"],
        "A_wqkv": _A_in["wqkv"],
        "A_wo": _A_in["wo"],
        "B_x": _B_in["x"],
        "B_w_gate": _B_in["w_gate"],
        "B_w_up": _B_in["w_up"],
        "B_w_down": _B_in["w_down"],
    }

def reference(A_x, A_wqkv, A_wo, B_x, B_w_gate, B_w_up, B_w_down):
    def _A_ref(x, wqkv, wo):
        B, S, D = x.shape
        H = 16; Dh = D // H
        W, dil = 8, 2
        qkv = (lambda x, *d: x.permute(*d))((x @ wqkv).reshape(B, S, 3, H, Dh), 2, 0, 3, 1, 4)
        q, k, v = qkv[0], qkv[1], qkv[2]
        scale = Dh ** -0.5
        pos = torch.arange(S)
        off = torch.arange(W) * dil
        idx = pos[:, None] - off[None, :]
        mask = idx >= 0
        idx_c = torch.where(mask, idx, 0).reshape(-1)
        kg = k[:, :, idx_c, :].reshape(B, H, S, W, Dh)
        vg = v[:, :, idx_c, :].reshape(B, H, S, W, Dh)
        logits = torch.einsum("bhsd,bhswd->bhsw", q, kg) * scale
        logits = torch.where(mask[None, None, :, :], logits, -1e9)
        attn = _softmax(logits, axis=-1)
        o = torch.einsum("bhsw,bhswd->bhsd", attn, vg)
        return (lambda x, *d: x.permute(*d))(o, 0, 2, 1, 3).reshape(B, S, D) @ wo

    def _B_ref(x, w_gate, w_up, w_down):
        g = _silu(x @ w_gate)
        u = x @ w_up
        return (g * u) @ w_down

    x = A_x
    xn = _layernorm(x, axis=-1)
    s = _A_ref(xn, A_wqkv, A_wo)
    g = _silu(_B_ref(xn, B_w_gate, B_w_up, B_w_down))
    return x + s * g
```

```cpp
#include <hip/hip_runtime.h>
#include <hip/hip_bf16.h>
typedef unsigned short u16;
__device__ __forceinline__ float bf2f(u16 b){ return __uint_as_float(((unsigned)b)<<16); }
__device__ __forceinline__ u16 f2bf(float f){ unsigned u=__float_as_uint(f); u += 0x7FFFu + ((u>>16)&1u); return (u16)(u>>16); }
__device__ __forceinline__ float rbf(float f){ return bf2f(f2bf(f)); }
constexpr int S=2048, D=448, F=1792, H=16, DH=28;

__global__ void k_ln(const u16* x, u16* xn){
  int row=blockIdx.x; int t=threadIdx.x;
  float v[7]; float s=0;
  for(int i=0;i<7;i++){ v[i]=bf2f(x[row*D+t+64*i]); s+=v[i]; }
  for(int o=32;o;o>>=1) s+=__shfl_xor(s,o);
  float mean=s/D; float q=0;
  for(int i=0;i<7;i++){ float d=v[i]-mean; q+=d*d; }
  for(int o=32;o;o>>=1) q+=__shfl_xor(q,o);
  float var=rbf(q/D); float m=rbf(mean);
  float sd=rbf(sqrtf(rbf(var+1e-5f)));
  for(int i=0;i<7;i++) xn[row*D+t+64*i]=f2bf(rbf(v[i]-m)/sd);
}
__global__ void k_gemm(const u16* A, const u16* B, u16* C, int M,int N,int K){
  int n=blockIdx.x*64+threadIdx.x; int m=blockIdx.y*4+threadIdx.y;
  if(n>=N) return;
  float acc=0;
  for(int k=0;k<K;k++) acc+=bf2f(A[m*K+k])*bf2f(B[k*N+n]);
  C[m*N+n]=f2bf(acc);
}
__global__ void k_attn(const u16* qkv, u16* o){
  int idx=blockIdx.x*blockDim.x+threadIdx.x; int s=idx/H, h=idx%H;
  const u16* q=qkv+s*3*D+h*DH;
  float l[8]; float mx=-1e30f;
  float scale=rbf(0.1889822365046136f);
  for(int w=0;w<8;w++){ int j=s-2*w; if(j<0){l[w]=-1e9f;} else { const u16* k=qkv+j*3*D+D+h*DH; float a=0; for(int d=0;d<DH;d++) a+=bf2f(q[d])*bf2f(k[d]); l[w]=rbf(rbf(a)*0.1889822365046136f);} mx=fmaxf(mx,l[w]); }
  float sum=0; for(int w=0;w<8;w++){ l[w]=__expf(l[w]-mx); sum+=l[w]; }
  for(int w=0;w<8;w++) l[w]=rbf(l[w]/sum);
  for(int d=0;d<DH;d++){ float a=0; for(int w=0;w<8;w++){ int j=s-2*w; if(j<0) j=0; a+=l[w]*bf2f(qkv[j*3*D+2*D+h*DH+d]); } o[s*D+h*DH+d]=f2bf(a); }
}
__global__ void k_silumul(const u16* g, const u16* u, u16* h, int n){
  int i=blockIdx.x*blockDim.x+threadIdx.x; if(i>=n) return;
  float a=bf2f(g[i]); float s=rbf(a/(1.f+__expf(-a))); h[i]=f2bf(s*bf2f(u[i]));
}
__global__ void k_final(const u16* x, const u16* s, const u16* y, u16* out, int n){
  int i=blockIdx.x*blockDim.x+threadIdx.x; if(i>=n) return;
  float a=bf2f(y[i]); float g=rbf(a/(1.f+__expf(-a))); out[i]=f2bf(bf2f(x[i])+rbf(bf2f(s[i])*g));
}
extern "C" void kernel_launch(void* const* d_in, const int* in_sizes, int n_in, void* d_out, int out_size, void* d_ws, size_t ws_size, hipStream_t stream){
  const u16* x=(const u16*)d_in[0]; const u16* wqkv=(const u16*)d_in[1]; const u16* wo=(const u16*)d_in[2];
  const u16* wg=(const u16*)d_in[4]; const u16* wu=(const u16*)d_in[5]; const u16* wd=(const u16*)d_in[6];
  u16* ws=(u16*)d_ws; u16* xn=ws; u16* qkv=xn+S*D; u16* o=qkv+S*3*D; u16* sb=o+S*D; u16* g=sb+S*D; u16* u=g+S*F; u16* h=u+S*F; u16* y=h+S*F;
  k_ln<<<S,64,0,stream>>>(x,xn);
  k_gemm<<<dim3(3*D/64,S/4),dim3(64,4),0,stream>>>(xn,wqkv,qkv,S,3*D,D);
  k_attn<<<S*H/256,256,0,stream>>>(qkv,o);
  k_gemm<<<dim3(D/64,S/4),dim3(64,4),0,stream>>>(o,wo,sb,S,D,D);
  k_gemm<<<dim3(F/64,S/4),dim3(64,4),0,stream>>>(xn,wg,g,S,F,D);
  k_gemm<<<dim3(F/64,S/4),dim3(64,4),0,stream>>>(xn,wu,u,S,F,D);
  k_silumul<<<S*F/256,256,0,stream>>>(g,u,h,S*F);
  k_gemm<<<dim3(D/64,S/4),dim3(64,4),0,stream>>>(h,wd,y,S,D,F);
  k_final<<<S*D/256,256,0,stream>>>(x,sb,y,(u16*)d_out,S*D);
}
```

```cpp
#include <hip/hip_runtime.h>
#include <hip/hip_bf16.h>

typedef __bf16 bf16x2 __attribute__((ext_vector_type(2)));
typedef __bf16 bf16x4 __attribute__((ext_vector_type(4)));
typedef __bf16 bf16x8 __attribute__((ext_vector_type(8)));
typedef float f32x2 __attribute__((ext_vector_type(2)));
typedef float f32x4 __attribute__((ext_vector_type(4)));
typedef float f32x16 __attribute__((ext_vector_type(16)));
typedef unsigned u32x2 __attribute__((ext_vector_type(2)));
typedef unsigned u32x4 __attribute__((ext_vector_type(4)));
typedef unsigned u32x16 __attribute__((ext_vector_type(16)));
typedef unsigned long long u64;
#define LAS __attribute__((address_space(3)))
#define DI __device__ __forceinline__
#define AI __attribute__((always_inline))

namespace {
constexpr int SEQ = 2048, DM = 448, FF = 1792;
constexpr unsigned PX = 896, PQ = 2688, PG = 3584;
constexpr unsigned FS = 8;
constexpr unsigned WS_FLAG = 256, FL_STRIDE = 160 * FS, FL_H = 0, FL_Q = 64 * FS, FL_O = 128 * FS;
constexpr unsigned WS_STAT = 16384 + 64 * FL_STRIDE;
constexpr unsigned WS_QKV = WS_STAT + 32768, QKV_TG = 168 * 256;
constexpr unsigned WS_HO = WS_QKV + 128 * QKV_TG, HO_T = 160 * 1024;
constexpr unsigned WS_END = WS_HO + 64 * HO_T;
constexpr unsigned LDS_MISC = 0;
constexpr unsigned SPIN_CAP = 1u << 17;
static_assert(WS_FLAG + 64 * FL_STRIDE <= WS_QKV, "flags");

constexpr unsigned DROP = 0x40000000u;
struct Tab { unsigned v[2048][16]; };
constexpr Tab make_tab() {
  Tab t{};
  for (unsigned b = 0; b < 256; ++b)
    for (unsigned w = 0; w < 8; ++w) {
      const unsigned xcd = b & 7, bi = b >> 3, m = w & 3, nh = w >> 2;
      const unsigned r = 8 * (xcd >> 2) + (bi >> 2), c = 4 * (xcd & 3) + (bi & 3);
      const bool qq = c >= 13, has2 = bi < 28;
      const unsigned T32 = 4 * r + m, t7 = bi / 7, T64 = 4 * xcd + (t7 & 3), n7 = bi % 7, T2 = 2 * T64 + nh;
      const unsigned tg = 8 * r + w, tgp = tg ? tg - 1 : 0, p0 = (7 * c) >> 1;
      const unsigned pair_a = c < 12 ? 16 + c : 12, pair_b = c;
      const unsigned sh = WS_HO + T32 * HO_T + nh * 2048, f1 = WS_FLAG + T32 * FL_STRIDE;
      unsigned* v = t.v[b * 8 + w];
      v[0] = (bi * 5) & 15;
      v[1] = WS_QKV + 2 * T32 * QKV_TG + nh * 1024;
      v[4] = f1 + (qq ? FL_Q + (2 * (2 * c - 10) + nh) * FS : FL_H + (32 * nh + pair_a) * FS);
      v[2] = qq ? (c == 15 ? v[4] : v[4] + 2 * FS) : sh + pair_a * 4096;
      v[3] = c == 12 ? DROP : sh + pair_b * 4096;
      v[5] = f1 + FL_Q + (2 * c + nh) * FS;
      v[6] = c == 12 ? DROP : f1 + FL_H + (32 * nh + pair_b) * FS;
      v[7] = WS_FLAG + (tg >> 1) * FL_STRIDE + FL_O + (2 * c + (tg & 1)) * FS;
      v[8] = WS_FLAG + T2 * FL_STRIDE;
      v[9] = WS_QKV + tg * QKV_TG + p0 * 256;
      v[10] = WS_QKV + tgp * QKV_TG + p0 * 256;
      v[11] = WS_HO + (tg >> 1) * HO_T + (128 + 2 * c) * 1024 + (tg & 1) * 256;
      v[12] = T2 * 32 * PX + n7 * 128 + m * 32;
      v[13] = v[0] << 12;
      v[14] = has2 ? n7 * 128 : DROP;
      v[15] = has2 ? WS_HO + T2 * HO_T + m * 1024 : DROP;
    }
  return t;
}
__constant__ const Tab kTab = make_tab();

constexpr u64 win_mask(int kt, int e) {
  u64 m = 0;
  for (int l = 0; l < 64; ++l) { const int q = l & 15, d = q + 16 - (16 * kt + 4 * (l >> 4) + 2 * e + (q & 1)); if (d >= 0 && d <= 14) m |= 1ull << l; }
  return m;
}
DI float maxf(float a, float b) { float r; asm("v_max_f32 %0, %1, %2" : "=v"(r) : "v"(a), "v"(b)); return r; }
DI float max3f(float a, float b, float c) { float r; asm("v_max3_f32 %0, %1, %2, %3" : "=v"(r) : "v"(a), "v"(b), "v"(c)); return r; }
DI float selm(float f, float t, u64 m) { float r; asm("v_cndmask_b32 %0, %1, %2, %3" : "=v"(r) : "v"(f), "v"(t), "s"(m)); return r; }

typedef u32x4 rsrc_t;
DI rsrc_t mk(unsigned lo, unsigned hi, unsigned bytes) { return rsrc_t{lo, hi & 0xffffu, bytes, 0x00020000u}; }
template <class T> DI void pins(T& a) { asm volatile("" : "+s"(a)); }

DI void dma(rsrc_t rs, unsigned voff, unsigned soff, unsigned lds, int imm) {
  asm volatile("s_add_u32 m0, %0, %4\n\ts_nop 0\n\tbuffer_load_dwordx4 %1, %2, %3 offen lds" ::"s"(lds), "v"(voff), "s"(rs), "s"(soff), "n"(imm) : "memory", "scc");
}
DI void dmax(rsrc_t rs, unsigned voff, unsigned x, int k, int mul, unsigned lds, int imm) {
  unsigned t;
  asm volatile("s_add_u32 m0, %1, %7\n\ts_xor_b32 %0, %4, %5\n\ts_mul_i32 %0, %0, %6\n\tbuffer_load_dwordx4 %2, %3, %0 offen lds"
               : "=&s"(t) : "s"(lds), "v"(voff), "s"(rs), "s"(x), "n"(k), "n"(mul), "n"(imm) : "memory", "scc");
}
DI void dmam(rsrc_t rs, unsigned voff, unsigned x, int mul, unsigned lds, int imm) {
  unsigned t;
  asm volatile("s_add_u32 m0, %1, %6\n\ts_mul_i32 %0, %4, %5\n\tbuffer_load_dwordx4 %2, %3, %0 offen lds"
               : "=&s"(t) : "s"(lds), "v"(voff), "s"(rs), "s"(x), "n"(mul), "n"(imm) : "memory", "scc");
}
DI u32x4 ld16(rsrc_t rs, unsigned voff, unsigned soff) {
  u32x4 r; asm volatile("buffer_load_dwordx4 %0, %1, %2, %3 offen" : "=v"(r) : "v"(voff), "s"(rs), "s"(soff) : "memory"); return r;
}
DI void csum(unsigned sel, int S, u32x4 one, u32x4 f0, u32x4 f1, f32x4& a0, f32x4& a1) {
  asm volatile("s_cmp_lg_u32 %5, %6\n\ts_cbranch_scc1 2f\n\tv_mfma_f32_16x16x32_bf16 %0, %2, %3, %0\n\tv_mfma_f32_16x16x32_bf16 %1, %2, %4, %1\n2:"
               : "+v"(a0), "+v"(a1) : "v"(one), "v"(f0), "v"(f1), "s"(sel), "n"(S) : "scc");
}
template <int N> DI void touch(unsigned& d, rsrc_t rs, unsigned voff, unsigned soff) {
  asm volatile("s_mov_b64 exec, %4\n\tbuffer_load_dword %0, %1, %2, %3 offen\n\ts_mov_b64 exec, -1" : "+v"(d) : "v"(voff), "s"(rs), "s"(soff), "n"((1 << N) - 1) : "memory");
}
DI u32x4 ld16s(rsrc_t rs, unsigned voff, unsigned soff) {
  u32x4 r; asm volatile("buffer_load_dwordx4 %0, %1, %2, %3 offen sc1" : "=v"(r) : "v"(voff), "s"(rs), "s"(soff) : "memory"); return r;
}
DI u32x2 ld8s(rsrc_t rs, unsigned voff, unsigned soff) {
  u32x2 r; asm volatile("s_mov_b64 exec, 0xffff\n\tbuffer_load_dwordx2 %0, %1, %2, %3 offen sc1\n\ts_mov_b64 exec, -1" : "=v"(r) : "v"(voff), "s"(rs), "s"(soff) : "memory"); return r;
}
DI void st16s(rsrc_t rs, unsigned voff, unsigned soff, u32x4 v) {
  asm volatile("buffer_store_dwordx4 %0, %1, %2, %3 offen sc1\n\ts_nop 1" ::"v"(v), "v"(voff), "s"(rs), "s"(soff) : "memory");
}
DI void st16(rsrc_t rs, unsigned voff, unsigned soff, u32x4 v) {
  asm volatile("buffer_store_dwordx4 %0, %1, %2, %3 offen\n\ts_nop 1" ::"v"(v), "v"(voff), "s"(rs), "s"(soff) : "memory");
}
DI void st8s(rsrc_t rs, unsigned soff, u32x2 v) {
  asm volatile("s_mov_b64 exec, 1\n\tbuffer_store_dwordx2 %0, off, %1, %2 sc1\n\ts_mov_b64 exec, -1" ::"v"(v), "s"(rs), "s"(soff) : "memory");
}
template <int N> DI void wvm() { static_assert(N >= 0 && N < 64, "vmcnt"); asm volatile("s_waitcnt vmcnt(%0)" ::"n"(N) : "memory"); }
template <int N> DI void wvm1(u32x4& a) { asm volatile("s_waitcnt vmcnt(%1)" : "+v"(a) : "n"(N) : "memory"); }
#define SCHED() __builtin_amdgcn_sched_barrier(0)
template <int N> DI void bar() {
  static_assert(N >= 0 && N < 64, "vmcnt");
  SCHED();
  __builtin_amdgcn_s_waitcnt(0xC07F);
  asm volatile("s_waitcnt vmcnt(%0) lgkmcnt(0)\n\ts_barrier" ::"n"(N) : "memory");
  SCHED();
}
DI LAS char* opq(unsigned p, int off) { unsigned v; asm volatile("v_add_u32 %0, %2, %1" : "=v"(v) : "v"(p), "n"(off)); return (LAS char*)(size_t)v; }
DI LAS char* lxor(LAS char* p, unsigned x) { return (LAS char*)(size_t)((unsigned)(size_t)p ^ x); }
template <class T> DI void pin(T& a) { asm volatile("" : "+v"(a)); }
template <class T> DI void pin(T& a, T& b) { asm volatile("" : "+v"(a), "+v"(b)); }
template <class T> DI void pin(T& a, T& b, T& c, T& d) { asm volatile("" : "+v"(a), "+v"(b), "+v"(c), "+v"(d)); }
DI unsigned pk(float a, float b) { f32x2 f = {a, b}; return __builtin_bit_cast(unsigned, __builtin_convertvector(f, bf16x2)); }
DI float rbf(float a) { return __builtin_bit_cast(float, pk(a, a) & 0xffff0000u); }
DI float lo16(unsigned a) { return __builtin_bit_cast(float, a << 16); }
DI float hi16(unsigned a) { return __builtin_bit_cast(float, a & 0xffff0000u); }
DI u32x4 trfrag(LAS char* p0, LAS char* p1) {
  bf16x4 a = __builtin_amdgcn_ds_read_tr16_b64_v4bf16((LAS bf16x4*)p0);
  bf16x4 b = __builtin_amdgcn_ds_read_tr16_b64_v4bf16((LAS bf16x4*)p1);
  return __builtin_bit_cast(u32x4, __builtin_shufflevector(a, b, 0, 1, 2, 3, 4, 5, 6, 7));
}
DI f32x16 mma(u32x4 a, u32x4 b, f32x16 c) {
  return __builtin_amdgcn_mfma_f32_32x32x16_bf16(__builtin_bit_cast(bf16x8, a), __builtin_bit_cast(bf16x8, b), c, 0, 0, 0);
}
DI f32x4 mma16(u32x4 a, u32x4 b, f32x4 c) {
  return __builtin_amdgcn_mfma_f32_16x16x32_bf16(__builtin_bit_cast(bf16x8, a), __builtin_bit_cast(bf16x8, b), c, 0, 0, 0);
}
DI float xchg16(float v, bool do_max) {
  const unsigned u = __builtin_bit_cast(unsigned, v);
  auto r = __builtin_amdgcn_permlane16_swap(u, u, false, false);
  const unsigned ra = r[0], rb = r[1];
  const float a = __builtin_bit_cast(float, ra), b = __builtin_bit_cast(float, rb);
  return do_max ? maxf(a, b) : a + b;
}
DI float xchg32(float v, bool do_max) {
  const unsigned u = __builtin_bit_cast(unsigned, v);
  auto r = __builtin_amdgcn_permlane32_swap(u, u, false, false);
  const unsigned ra = r[0], rb = r[1];
  const float a = __builtin_bit_cast(float, ra), b = __builtin_bit_cast(float, rb);
  return do_max ? maxf(a, b) : a + b;
}
DI float swi(float g, float u, float nrl, float ir2) {
  const float e = __builtin_amdgcn_exp2f(g * nrl);
  return (g * u) * __builtin_amdgcn_rcpf(__builtin_fmaf(e, ir2, ir2));
}
DI bool flags_ok(u32x2 v, u32x2 tag, u64 mask) { return (__builtin_amdgcn_ballot_w64(__builtin_bit_cast(u64, v) != __builtin_bit_cast(u64, tag)) & mask) == 0ull; }
DI void spin(rsrc_t ws, unsigned voff, unsigned soff, u32x2 tag, u64 mask) {
#pragma unroll 1
  for (unsigned it = 0; it < SPIN_CAP; ++it) {
    u32x2 v;
    asm volatile("s_mov_b64 exec, %4\n\tbuffer_load_dwordx2 %0, %1, %2, %3 offen sc1\n\ts_mov_b64 exec, -1\n\ts_waitcnt vmcnt(0)" : "=v"(v) : "v"(voff), "s"(ws), "s"(soff), "s"(mask) : "memory");
    if (flags_ok(v, tag, mask)) break;
    __builtin_amdgcn_s_sleep(1);
  }
}
DI float dot2(unsigned a, unsigned b, float c) {
  return __builtin_amdgcn_fdot2_f32_bf16(__builtin_bit_cast(bf16x2, a), __builtin_bit_cast(bf16x2, b), c, false);
}
template <int CTRL> DI float dppf(float v) {
  return __builtin_bit_cast(float, __builtin_amdgcn_update_dpp(0, __builtin_bit_cast(int, v), CTRL, 0xf, 0xf, true));
}
DI float sum64(float v) { v += dppf<0xB1>(v); v += dppf<0x4E>(v); v += dppf<0x141>(v); v += dppf<0x140>(v); return xchg32(xchg16(v, false), false); }
}

#if defined(__HIP_DEVICE_COMPILE__)
template <bool qq>
DI void body(LAS char* const sm) {
  u32x16 ka;
  asm volatile("s_load_dwordx16 %0, %1, 0x0 ; variant %c2" : "=s"(ka) : "s"(__builtin_amdgcn_kernarg_segment_ptr()), "n"((int)qq));
  unsigned dmy = 0;
  {
    u64 pc; unsigned len;
    asm volatile("s_getpc_b64 %0\n.Lfk_pc%c2:\n\ts_mov_b32 %1, .Lfk_end%c2-.Lfk_pc%c2" : "=s"(pc), "=s"(len) : "n"((int)qq));
    touch<4>(dmy, mk((unsigned)pc, (unsigned)(pc >> 32), len), (threadIdx.x >> 6) * 4096 + (threadIdx.x & 3) * 128, (blockIdx.x >> 5) * 512);
  }
#define gx ka[0], ka[1]
#define gwqkv ka[2], ka[3]
#define gwo ka[4], ka[5]
#define gwg ka[6], ka[7]
#define gwu ka[8], ka[9]
#define gwd ka[10], ka[11]
#define gout ka[12], ka[13]
#define gws ka[14], ka[15]
  const unsigned smb = (unsigned)(size_t)sm;
  unsigned tid = threadIdx.x, b = blockIdx.x;
  asm volatile("; variant %c1" : "+v"(tid) : "n"((int)qq)); asm volatile("; variant %c1" : "+s"(b) : "n"((int)qq));
  const unsigned l = tid & 63;
  unsigned w = __builtin_amdgcn_readfirstlane(tid >> 6);
  unsigned xcd = b & 7, bi = b >> 3;
  unsigned r = 8 * (xcd >> 2) + (bi >> 2), c = 4 * (xcd & 3) + (bi & 3);
  const unsigned krot = bi >> 2;
  const unsigned kotab = (unsigned)(0x6543210654321065ull >> (4 * krot));
  auto ko = [&](unsigned j) AI -> unsigned { return (kotab >> (4 * j)) & 7; };
  const unsigned l3 = l >> 3, l7 = l & 7;
  constexpr unsigned pa = qq ? PQ : PG;
  const unsigned prow = 8 * w + l3;
  const unsigned la0 = 2 * c - 10, la1 = c == 15 ? 20 : 2 * c - 9;
  const unsigned pra = c < 12 ? 16 + c : 12;
  unsigned kx[7];
  kx[0] = ko(0) * 128;
  unsigned sw0 = (l7 ^ ((l3 & 2) << 1)) * 16;
  unsigned vx = (r * 128 + prow) * PX + ((l7 ^ (4 * (w & 1) + (l >> 4))) * 16), vx1 = vx + 64 * PX;
  unsigned va0 = prow * pa + sw0 + (qq ? la0 : pra) * 128, va1 = prow * pa + (sw0 ^ 32) + (qq ? la1 : pra) * 128;
  unsigned vst = l * 16, vrow = l < 56 ? vst : DROP, srow = r * 128 + c * 8 + w;
  unsigned ldsw = smb + w * 1024;
  pin(vx, vx1); pin(va0, va1); pin(vst, vrow); pin(sw0); pins(srow); pins(ldsw); pins(kx[0]);
  SCHED();
  asm volatile("s_waitcnt lgkmcnt(0)" : "+s"(ka));
  rsrc_t rx = mk(gx, SEQ * PX), rws = mk(gws, WS_END);
  rsrc_t rq = mk(gwqkv, DM * PQ), rg = mk(gwg, DM * PG), ru = mk(gwu, DM * PG);
  auto issueX = [&](int j, int h, int s) AI { dma(rx, h ? vx1 : vx, kx[j], ldsw, s * 8192); };
  auto issueA = [&](int j, int h, int s) AI { dmam(qq ? rq : (h ? ru : rg), h ? va1 : va0, kx[j], pa / 2, ldsw, s * 8192); };
  u32x2 tag;
  u32x4 xrw = ld16(rx, vrow, srow * PX);
  asm volatile("s_mov_b64 exec, 1\n\tbuffer_load_dwordx2 %0, off, %1, 0 sc1\n\ts_mov_b64 exec, -1" : "=v"(tag) : "s"(rws) : "memory");
  issueX(0, 0, 2); issueX(0, 1, 3); issueA(0, 0, 4); issueA(0, 1, 5);
  SCHED();
  pins(c); pins(w); pin(tid);
  const unsigned m = w & 3, nh = w >> 2;
  const unsigned kg = m, mh = nh;
  const unsigned tok = l & 31, hh = l >> 5, blk = (l >> 4) & 1, qr = (l & 15) >> 2, pp = l & 3, g4 = l >> 4;
  const unsigned fq = qr & 2;
#pragma unroll
  for (int j = 1; j < 7; ++j) { kx[j] = ko(j) * 128; pins(kx[j]); }
  const unsigned s1 = qr == g4 ? 0x3F803F80u : 0u;
  u32x4 sel = {s1, s1, s1, s1};
  unsigned csel = (m + 2 * nh) & 3;
  unsigned tF0 = smb + blk * 8192 + (8 * hh + qr) * 128 + (((2 * nh) ^ fq ^ blk) * 32) + pp * 8;
  unsigned tA = smb + (32 * m + tok) * 128 + ((hh ^ (tok >> 1)) & 7) * 16;
  pins(rq); pins(rg); pins(ru); pins(rws); pin(sel); pin(tF0, tA); pins(csel);
  SCHED();
  constexpr u64 Y_SLOT = 0x0b0a090807060201ull, S_SLOT = 0x0f0e0d0c05040300ull;
  constexpr int V_SLOT = 19;
  u32x16 tb;
  rsrc_t rd, ro;
  unsigned vq, vg, vu, tL, t20, vqs, vd, vo, vh, vpa, vpq, csw, vgs, tgh, mlo, mhi, avw, avr, avo, apm;
#define xr_ tb[0]
#define sq tb[1]
#define sha tb[2]
#define shb tb[3]
#define sfa tb[4]
#define sfq tb[5]
#define sfb tb[6]
#define sfo tb[7]
#define sf2 tb[8]
#define sat tb[9]
#define satp tb[10]
#define sos tb[11]
#define sout tb[12]
#define x12 tb[13]
#define cn_ tb[14]
#define shh tb[15]
  unsigned xs[3], xh[3];
  const unsigned tg = 8 * r + w;
  const bool has2 = b < 224;
  auto setup = [&](int k) AI {
    if (k == 0) { rd = mk(gwd, FF * PX); pins(rd); csw = smb + LDS_MISC + nh * 2048 + vst; pin(csw); }
    if (k == 1) { vq = prow * PQ + sw0 + c * 128; pin(vq); }
    if (k == 2) { asm volatile("" : "+s"(tb)); vd = prow * PX + sw0 + cn_; pin(vd); xs[0] = xr_; xs[1] = xr_ >> 1; xs[2] = xr_ >> 2; pins(xs[1]); pins(xs[2]); }
    if (k == 3) { vg = prow * PG + sw0 + c * 128; vu = vg ^ 32; pin(vg, vu); }
    if (k == 4) { ro = mk(gwo, DM * PX); pins(ro); vgs = tok * 16 + hh * 8; tgh = hh ? tag.y : tag.x; pin(vgs, tgh); }
    if (k == 5) { tL = tF0 & ~8192u; pin(tL); vh = vst + shh; pin(vh); xh[0] = x12; xh[1] = xs[1] << 12; xh[2] = xs[2] << 12; pins(xh[1]); pins(xh[2]); }
    if (k == 6) { t20 = smb + (16 * m + 4 * hh + qr) * 128 + ((blk ^ fq ^ m) * 32) + pp * 8; pin(t20); }
    if (k == 7) { vqs = (tok >> 4) * QKV_TG + hh * 256 + (tok & 15) * 16; pin(vqs); }
    if (k == 8) { vo = (24 * (w >> 2) + 16 * (w & 1) + 8 * ((w >> 1) & 1) + l3) * PX + sw0 + cn_; pin(vo); }
    if (k == 9) { vpa = FL_H + (32 * (kg >> 1) + 16 + (l < 12 ? l : 0)) * FS; pin(vpa); }
    if (k == 10) { vpq = (14 * (qr < 3 ? qr : 0) + ((((7 * c) >> 1) + 3 * (l & 1)) >> 2)) * FS; pin(vpq); }
    if (k == 11) { vpq += WS_FLAG + FL_Q + (((l & 2) ? tg - ((tg + 127) >> 7) : tg) >> 1) * FL_STRIDE; pin(vpq); }
    if (k == 12) { mlo = ((c & 1) && g4 == 0) ? 0u : ~0u; mhi = (!(c & 1) && g4 == 3) ? 0u : ~0u; pin(mlo, mhi); }
    if (k == 13) { avw = ldsw + V_SLOT * 8192 + vst; avr = ldsw + V_SLOT * 8192 + (pp >> 1) * 256 + (4 * g4 + qr) * 16 + (pp & 1) * 8; pin(avw, avr); }
    if (k == 14) { avo = g4 * 512 + (l & 15) * 16; apm = (l & 1) ? 0xffff0000u : 0xffffu; pin(avo, apm); }
  };
#define TOUCH_W2() { const unsigned u = b * 8 + w; touch<8>(dmy, u < 1568 ? rd : ro, vst * 8, (u < 1568 ? u : u - 1568) * 1024); }
  auto pbase = [](int i) AI -> int { return i < 12 ? 16 + i : (i < 28 ? i - 12 : i + 4); };
  auto pshift = [](int i) AI -> int { return i < 8 ? 1 : (i < 12 ? 2 : (i < 28 ? 0 : 1)); };
  auto issueQ = [&](int j, int s) AI { dmam(rq, vq, kx[j], PQ / 2, ldsw, s * 8192); };
  auto issueB = [&](int j, int h, int s) AI { dmam(h ? ru : rg, h ? vu : vg, kx[j], PG / 2, ldsw, s * 8192); };
  auto issueW = [&](int i, int s) AI { dmax(i < 28 ? rd : ro, i < 28 ? vd : vo, xs[pshift(i)], pbase(i) & 31, i < 28 ? 64 * PX : 56 * PX, ldsw, s * 8192); };
  asm volatile("s_load_dwordx16 %0, %1, %2" : "=s"(tb) : "s"(&kTab), "s"((b * 8 + w) * 64));
  SCHED();

#define WAIT_ROW(N) asm volatile("s_waitcnt vmcnt(%3)" : "+v"(xrw), "+v"(tag), "+v"(dmy) : "n"(N) : "memory")
  auto row_stats = [&]() AI {
    u64 e = ((u64)(unsigned)__builtin_amdgcn_readfirstlane(tag.y) << 32) | (unsigned)__builtin_amdgcn_readfirstlane(tag.x);
    e = e * 6364136223846793005ull + 1442695040888963407ull; tag.x = (unsigned)e; tag.y = (unsigned)(e >> 32); pin(tag);
    const float mean = sum64(dot2(xrw.x, 0x3F803F80u, dot2(xrw.y, 0x3F803F80u, 0.f)) + dot2(xrw.z, 0x3F803F80u, dot2(xrw.w, 0x3F803F80u, 0.f))) * (1.0f / DM);
    const float ml = l < 56 ? mean : 0.f;
    float q = 0.f;
#pragma unroll
    for (int e2 = 0; e2 < 4; ++e2) { const unsigned v = xrw[e2]; const float a = lo16(v) - ml, b_ = hi16(v) - ml; q = __builtin_fmaf(a, a, __builtin_fmaf(b_, b_, q)); }
    const float sd = __builtin_amdgcn_sqrtf(rbf(rbf(sum64(q) * (1.0f / DM)) + 1e-5f));
    const u32x2 gr = {pk(-mean, sd), (l & 1) ? tag.y : tag.x};
    asm volatile("s_mov_b64 exec, 3\n\tbuffer_store_dwordx2 %0, %1, %2, %3 offen sc1\n\ts_mov_b64 exec, -1" ::"v"(gr), "v"(l * 8), "s"(rws), "s"(WS_STAT + srow * 16) : "memory");
  };
#define BAR0(N) bar<N>()
  SCHED();

  constexpr int LEAD = 3;
  f32x16 accq = {}, acca[2] = {}, accb[2] = {};
  float rs, nrl, r2;
  u32x4 A[28];
  u32x4 Bf[LEAD + 1][2];
  u32x4 Hf[36], Wf[36][2];
  LAS char *tb0, *tb1;
  f32x16 y[2] = {}, sa[2] = {};
  u32x2 gst;
  f32x4 cs0 = {}, cs1 = {};
  u32x4 nmf, csf0, csf1;
#define TOUCH_DONE(N) asm volatile("s_waitcnt vmcnt(%1)" : "+v"(dmy) : "n"(N) : "memory")
#define LOAD_STAT() asm volatile("buffer_load_dwordx2 %0, %1, %2, %3 offen sc1" : "=v"(gst) : "v"(vgs), "s"(rws), "s"(WS_STAT + (r * 128 + m * 32) * 16) : "memory")
#define GET_STAT(N) { asm volatile("s_waitcnt vmcnt(%1)" : "+v"(gst) : "n"(N) : "memory");                                            \
    _Pragma("unroll 1") for (unsigned it = 0; it < SPIN_CAP && __builtin_amdgcn_ballot_w64(gst.y != tgh) != 0ull; ++it) {             \
      __builtin_amdgcn_s_sleep(1);                                                                                                    \
      asm volatile("buffer_load_dwordx2 %0, %1, %2, %3 offen sc1\n\ts_waitcnt vmcnt(0)" : "+v"(gst) : "v"(vgs), "s"(rws), "s"(WS_STAT + (r * 128 + m * 32) * 16) : "memory"); } \
    const float sd = hi16(gst.x); rs = __builtin_amdgcn_rcpf(sd); nrl = -1.4426950408889634f * rs; r2 = sd * sd;                      \
    const unsigned nm = __builtin_amdgcn_perm(gst.x, gst.x, 0x01000100u); nmf = u32x4{nm, nm, nm, nm}; pin(rs, nrl); pin(r2); pin(nmf); }
  auto cs_step = [&](int i) AI { csum(csel, i & 3, sel, Bf[i % (LEAD + 1)][0], Bf[i % (LEAD + 1)][1], cs0, cs1); };
  auto put_cs = [&](bool both) AI {
    asm volatile("s_nop 7" : "+v"(cs0), "+v"(cs1));
    const float p0_ = both ? cs0[0] : cs0[0] + cs1[0], p1_ = cs1[0];
    *(LAS unsigned*)((LAS char*)(size_t)csw + m * 4) = pk(p0_, p0_ - rbf(p0_));
    if (both) *(LAS unsigned*)((LAS char*)(size_t)csw + 1024 + m * 4) = pk(p1_, p1_ - rbf(p1_));
    cs0 = f32x4{}; cs1 = f32x4{}; asm volatile("s_nop 1" : "+v"(cs0), "+v"(cs1));
  };
#define CORRECT(ACC, T) { ACC = mma(*(LAS u32x4*)((LAS char*)(size_t)csw + (T) * 1024), nmf, ACC); pin(ACC); }
#define LOAD_CS2() { csf0 = *(LAS u32x4*)(size_t)csw; csf1 = *(LAS u32x4*)((LAS char*)(size_t)csw + 1024); }
#define CORRECT2() { accb[0] = mma(csf0, nmf, accb[0]); accb[1] = mma(csf1, nmf, accb[1]); pin(accb[0], accb[1]); }
  LAS char* tac;
  auto readA = [&](int s, int sl) AI {
    if ((s & 3) == 0) tac = opq(tA, sl * 8192);
    A[s] = *(LAS u32x4*)lxor(tac, 32u * (s & 3));
  };
  auto readB = [&](int i, int sl) AI {
    const bool qs = i >= 28 && i < 42;
    const int s = i < 28 ? (i & 3) : (qs ? (i & 1) : ((i - 42) & 3));
    u32x4* B = Bf[i % (LEAD + 1)];
    if (s == 0) { if (qs) { tb0 = opq(tL, sl * 8192); tb1 = tb0 + 2048; } else { tb0 = opq(tF0, sl * 8192); tb1 = lxor(tb0, 32u); } }
    const int o = qs ? s * 4096 : s * 2048;
    B[0] = trfrag(tb0 + o, tb0 + o + 512);
    B[1] = trfrag(tb1 + o, tb1 + o + 512);
  };
  auto compute = [&](int i) AI {
    u32x4* B = Bf[i % (LEAD + 1)];
    if (i < 28) { acca[0] = mma(B[0], A[i], acca[0]); acca[1] = mma(B[1], A[i], acca[1]); pin(acca[0], acca[1]); }
    else if (i < 42) { accq = mma(B[0], A[2 * (i - 28)], accq); accq = mma(B[1], A[2 * (i - 28) + 1], accq); pin(accq); }
    else { accb[0] = mma(B[0], A[i - 42], accb[0]); accb[1] = mma(B[1], A[i - 42], accb[1]); pin(accb[0], accb[1]); }
  };
  u32x4 hp;
  auto hpart = [&](const f32x16* C, int t, int e) AI -> unsigned {
    return pk(swi(C[t][2 * e], C[t][8 + 2 * e], nrl, r2), swi(C[t][2 * e + 1], C[t][9 + 2 * e], nrl, r2));
  };
  float hv;
  auto hone = [&](const f32x16* C, int e) AI {
    const int t = e >> 3, j = e & 7;
    float v = swi(C[t][j], C[t][8 + j], nrl, r2);
    if (j & 1) { unsigned p = pk(hv, v); pin(p); hp[j >> 1] = p; } else { pin(v); hv = v; }
  };
  u32x2 pol;
  unsigned vpol, spol;
  u32x4 qp;
  auto qpart = [&](const f32x16& cc, int e, u32x4& d) AI {
    const int o = 8 * (e >> 1) + 2 * (e & 1);
    auto r0 = __builtin_amdgcn_permlane32_swap(pk(cc[o] * rs, cc[o + 1] * rs), pk(cc[o + 4] * rs, cc[o + 5] * rs), false, false);
    unsigned a = r0[0], b_ = r0[1]; pin(a, b_); d[e & 1] = a; d[2 + (e & 1)] = b_;
  };
  auto qhalf = [&](int e) AI { qpart(accq, e, qp); };
  auto epq = [&](int e) AI {
    qpart(acca[e >> 2], e & 3, hp);
    if (e & 1) st16s(rws, vqs, sq + ((e & 2) ? la1 : la0) * 2048 + (e >> 2) * 512, hp);
  };
  auto epa = [&](int e) AI { if (qq) { if (e & 1) epq(e >> 1); } else hone(acca, e); };
#define STORE_HA(T) if (!qq) st16s(rws, vst, sha + (T) * 1024, hp)
#define STORE_Q(T) st16s(rws, vqs, sq + c * 2048 + (T) * 512, qp)
#define STORE_HB(T) { _Pragma("unroll") for (int e = 0; e < 4; ++e) hp[e] = hpart(accb, T, e); st16s(rws, vst, shb + (T) * 1024, hp); }
#define FLAG_A() st8s(rws, sfa, tag)
#define FLAG_A2() if (qq) st8s(rws, sha, tag)
#define FLAG_Q() st8s(rws, sfq, tag)
#define FLAG_B() st8s(rws, sfb, tag)
#define FLAG_O() st8s(rws, sfo, tag)
#define POLL_A() { vpol = vpa; spol = sf2; pol = ld8s(rws, vpol, spol); }
#define POLL_B() { vpol = FL_H + (32 * (kg >> 1) + l) * FS; spol = sf2; pol = ld8s(rws, vpol, spol); }
#define POLL_O() { vpol = (FL_O + (4 * (l >> 1) + 2 * (kg >> 1) + (l & 1)) * FS); spol = sf2; pol = ld8s(rws, vpol, spol); }
  u32x2 polq;
#define POLL_Q() polq = ld8s(rws, vpq, 0u)
#define CHECK_Q(N) { asm volatile("s_waitcnt vmcnt(%1)" : "+v"(polq) : "n"(N) : "memory"); if (!flags_ok(polq, tag, 0xffffull)) spin(rws, vpq, 0u, tag, 0xffffull); }
#define CHECK_POLL(N) { asm volatile("s_waitcnt vmcnt(%1)" : "+v"(pol) : "n"(N) : "memory"); if (!flags_ok(pol, tag, 0xffffull)) spin(rws, vpol, spol, tag, 0xffffull); }
  auto ldH = [&](int i) AI { Hf[i] = ld16s(rws, vh, xh[pshift(i)] ^ (pbase(i) * 4096)); };
  LAS char* tw[3][2];
  bool have_tw = false;
#define MAKE_TW() { tw[0][0] = (LAS char*)(size_t)t20; tw[1][0] = opq(t20, 65536); tw[2][0] = opq(t20, 131072);                       \
    _Pragma("unroll") for (int k = 0; k < 3; ++k) { unsigned v = (unsigned)(size_t)tw[k][0] ^ 64u; pin(v); tw[k][1] = (LAS char*)(size_t)v; } have_tw = true; }
  auto readW = [&](int i, int sl) AI {
    LAS char *a0, *a1;
    if (have_tw) { a0 = tw[sl >> 3][0] + (sl & 7) * 8192; a1 = tw[sl >> 3][1] + (sl & 7) * 8192; }
    else { a0 = opq(t20, sl * 8192); a1 = lxor(a0, 64u); }
    Wf[i][0] = trfrag(a0, a0 + 1024);
    Wf[i][1] = trfrag(a1, a1 + 1024);
  };
  auto comp2 = [&](int i) AI {
    if (i < 28) { y[0] = mma(Wf[i][0], Hf[i], y[0]); y[1] = mma(Wf[i][1], Hf[i], y[1]); pin(y[0], y[1]); }
    else { sa[0] = mma(Wf[i][0], Hf[i], sa[0]); sa[1] = mma(Wf[i][1], Hf[i], sa[1]); pin(sa[0], sa[1]); }
  };
  u32x4 at[5], of, xe;
#define LOAD_ATT(K) at[K] = ld16s(rws, vst, (((K) & 1) ? satp : sat) + 56 * 256 * (((K) + 1) >> 1))
#define WAIT_ATT(N) asm volatile("s_waitcnt vmcnt(%5)" : "+v"(at[0]), "+v"(at[1]), "+v"(at[2]), "+v"(at[3]), "+v"(at[4]) : "n"(N) : "memory")
  f32x4 as0, as1, ao0, ao1;
  bf16x4 v00, v01, v10, v11;
  float cd[4], amx, asum;
  u32x4 pf;
  auto att = [&](int k) AI {
    constexpr float cs = 0.1889822365046136f * 1.4426950408889634f;
    if (k == 0) {
      at[0] &= u32x4{mlo, mlo, mhi, mhi};
      as0 = mma16(at[1], at[0], f32x4{}); as1 = mma16(at[2], at[0], f32x4{});
      LAS char* vw = (LAS char*)(size_t)avw; LAS char* vr = (LAS char*)(size_t)avr;
      *(LAS u32x4*)vw = at[3];
      v00 = __builtin_amdgcn_ds_read_tr16_b64_v4bf16((LAS bf16x4*)vr); v01 = __builtin_amdgcn_ds_read_tr16_b64_v4bf16((LAS bf16x4*)(vr + 512));
      *(LAS u32x4*)vw = at[4];
      v10 = __builtin_amdgcn_ds_read_tr16_b64_v4bf16((LAS bf16x4*)vr); v11 = __builtin_amdgcn_ds_read_tr16_b64_v4bf16((LAS bf16x4*)(vr + 512));
      pin(as0, as1);
    }
    if (k == 1) {
      constexpr u64 odd = 0xAAAAAAAAAAAAAAAAull;
      const float neg = -3.0e38f;
      asm volatile("s_nop 7" : "+v"(as0), "+v"(as1));
      cd[0] = selm(neg, selm(as0[0], as0[1], odd), tg ? win_mask(0, 0) : 0ull); cd[1] = selm(neg, selm(as0[2], as0[3], odd), tg ? win_mask(0, 1) : 0ull);
      cd[2] = selm(neg, selm(as1[0], as1[1], odd), win_mask(1, 0)); cd[3] = selm(neg, selm(as1[2], as1[3], odd), win_mask(1, 1));
      amx = maxf(max3f(cd[0], cd[1], cd[2]), cd[3]);
      pin(cd[0], cd[1], cd[2], cd[3]); pin(amx);
    }
    if (k == 2) { amx = -cs * xchg32(xchg16(amx, true), true); pin(amx); }
    if (k == 3) {
#pragma unroll
      for (int j = 0; j < 4; ++j) cd[j] = __builtin_amdgcn_exp2f(__builtin_fmaf(cd[j], cs, amx));
      asum = (cd[0] + cd[1]) + (cd[2] + cd[3]);
      pin(cd[0], cd[1], cd[2], cd[3]); pin(asum);
    }
    if (k == 4) { asum = __builtin_amdgcn_rcpf(xchg32(xchg16(asum, false), false)); pin(asum); }
    if (k == 5) {
#pragma unroll
      for (int j = 0; j < 4; ++j) { const float p = cd[j] * asum; pf[j] = pk(p, p) & apm; }
      ao0 = mma16(__builtin_bit_cast(u32x4, __builtin_shufflevector(v00, v10, 0, 1, 2, 3, 4, 5, 6, 7)), pf, f32x4{});
      ao1 = mma16(__builtin_bit_cast(u32x4, __builtin_shufflevector(v01, v11, 0, 1, 2, 3, 4, 5, 6, 7)), pf, f32x4{});
      pin(ao0, ao1);
    }
    if (k == 6) of = u32x4{pk(ao0[0], ao0[1]) & mlo, pk(ao0[2], ao0[3]) & mlo, pk(ao1[0], ao1[1]) & mhi, pk(ao1[2], ao1[3]) & mhi};
  };
#define STORE_O() st16s(rws, avo, sos, of)
#define SPARE_EXIT() if (!has2) {                                                                                                   \
    if (b == 255) { _Pragma("unroll 1") for (unsigned i = 0; i < 4; ++i)                                                              \
        spin(rws, WS_FLAG + (8 * w + 2 * i + (l >> 5)) * FL_STRIDE + FL_O + (l & 31) * FS, 0u, tag, ~0ull);                                \
      bar<63>(); st8s(rws, w == 0 ? 0u : DROP, tag); }                                                                                \
    __builtin_amdgcn_endpgm(); }
#define LOAD_XE() { vxe = tok * PX + hh * 16; xe = ld16(rx, vxe, sout); }
  unsigned vxe;
  LAS char *pw, *pr[3];
  auto part_addr = [&](u64 S) AI {
    pw = (LAS char*)(size_t)(vst + ((unsigned)(S >> (8 * w)) & 255) * 8192);
#pragma unroll
    for (int v = 1; v < 4; ++v) { unsigned a = vst + ((unsigned)(S >> (8 * (w ^ v))) & 255) * 8192; pin(a); pr[v - 1] = (LAS char*)(size_t)a; }
  };
  auto put_part = [&](const f32x16* C, int k) AI {
    const int q = k + 2, ct = q >> 2, a = q & 3;
    *(LAS f32x4*)(pw + k * 1024) = f32x4{C[ct][4 * a], C[ct][4 * a + 1], C[ct][4 * a + 2], C[ct][4 * a + 3]};
  };
  auto get_part = [&](const f32x16* C, f32x4& v0, f32x4& v1) AI {
    f32x4 a[3], b_[3];
#pragma unroll
    for (int v = 0; v < 3; ++v) { a[v] = *(LAS f32x4*)(pr[v] + v * 2048); b_[v] = *(LAS f32x4*)(pr[v] + v * 2048 + 1024); }
    v0 = (f32x4{C[0][0], C[0][1], C[0][2], C[0][3]} + a[0]) + (a[1] + a[2]);
    v1 = (f32x4{C[0][4], C[0][5], C[0][6], C[0][7]} + b_[0]) + (b_[1] + b_[2]);
  };
  f32x4 y0, y1;
#define PRIO(N) __builtin_amdgcn_s_setprio(N)
#define ADDR_Y() part_addr(Y_SLOT)
#define ADDR_S() part_addr(S_SLOT)
#define PUT_Y(K) put_part(y, K)
#define PUT_S() { _Pragma("unroll") for (int k = 0; k < 6; ++k) put_part(sa, k); }
#define GET_Y0() { get_part(y, y0, y1); pin(y0, y1); }
#define GET_Y1() { _Pragma("unroll") for (int e = 0; e < 4; ++e) {                                                                    \
      y0[e] *= __builtin_amdgcn_rcpf(1.0f + __builtin_amdgcn_exp2f(-1.4426950408889634f * y0[e]));                                    \
      y1[e] *= __builtin_amdgcn_rcpf(1.0f + __builtin_amdgcn_exp2f(-1.4426950408889634f * y1[e])); } pin(y0, y1); }
#define FINAL() { f32x4 s0, s1; get_part(sa, s0, s1); s0 *= y0; s1 *= y1;                                                             \
    auto r0 = __builtin_amdgcn_permlane32_swap(pk(s0[0], s0[1]), pk(s1[0], s1[1]), false, false);                                     \
    auto r1 = __builtin_amdgcn_permlane32_swap(pk(s0[2], s0[3]), pk(s1[2], s1[3]), false, false);                                     \
    const u32x4 pv = {r0[0], r1[0], r0[1], r1[1]}; u32x4 ov;                                                                          \
    _Pragma("unroll") for (int e = 0; e < 4; ++e) { const unsigned xv = xe[e], p_ = pv[e]; ov[e] = pk(lo16(xv) + lo16(p_), hi16(xv) + hi16(p_)); } \
    st16(mk(gout, SEQ * PX), vxe, sout, ov); }

  setup(0); setup(1); setup(3); setup(6); SCHED();
  issueX(1, 0, 6); issueX(1, 1, 7); issueA(1, 0, 8); issueA(1, 1, 9); WAIT_ROW(8); row_stats(); issueX(2, 0, 10); issueX(2, 1, 11); issueA(2, 0, 12); issueA(2, 1, 13);
  BAR0(9);
  readB(0, 4); readA(0, 2); readB(1, 4); readA(1, 2); readB(2, 4); readA(2, 2); SCHED();
  readB(3, 4); readA(3, 2); compute(0); cs_step(0); cs_step(1); setup(2); issueX(3, 0, 14); issueX(3, 1, 15); SCHED();
  bar<7>();
  readB(4, 8); readA(4, 6); compute(1); cs_step(2); setup(5); issueA(3, 0, 2); issueA(3, 1, 3); SCHED();
  readB(5, 8); readA(5, 6); compute(2); cs_step(3); setup(7); issueX(4, 0, 4); issueX(4, 1, 5); SCHED();
  readB(6, 8); readA(6, 6); compute(3); cs_step(4); setup(8); issueA(4, 0, 16); issueA(4, 1, 17); SCHED();
  readB(7, 8); readA(7, 6); compute(4); cs_step(5); setup(9); issueX(5, 0, 18); SCHED();
  bar<9>();
  readB(8, 12); readA(8, 10); compute(5); cs_step(6); setup(10); issueX(5, 1, 19); SCHED();
  readB(9, 12); readA(9, 10); compute(6); cs_step(7); setup(11); issueA(5, 0, 6); SCHED();
  readB(10, 12); readA(10, 10); compute(7); cs_step(8); setup(4); issueA(5, 1, 7); SCHED();
  readB(11, 12); readA(11, 10); compute(8); cs_step(9); issueX(6, 0, 8); SCHED();
  bar<9>();
  readB(12, 2); readA(12, 14); compute(9); cs_step(10); issueX(6, 1, 9); SCHED();
  readB(13, 2); readA(13, 14); compute(10); cs_step(11); issueA(6, 0, 10); SCHED();
  readB(14, 2); readA(14, 14); compute(11); cs_step(12); issueA(6, 1, 11); SCHED();
  readB(15, 2); readA(15, 14); compute(12); cs_step(13); issueQ(0, 1); SCHED();
  bar<9>();
  readB(16, 16); readA(16, 4); compute(13); cs_step(14); issueQ(1, 2); SCHED();
  readB(17, 16); readA(17, 4); compute(14); cs_step(15); issueQ(2, 3); SCHED();
  readB(18, 16); readA(18, 4); compute(15); cs_step(16); issueQ(3, 12); SCHED();
  readB(19, 16); readA(19, 4); compute(16); cs_step(17); issueQ(4, 13); SCHED();
  bar<9>();
  readB(20, 6); readA(20, 18); compute(17); cs_step(18); issueQ(5, 4); SCHED();
  readB(21, 6); readA(21, 18); compute(18); cs_step(19); issueQ(6, 5); SCHED();
  readB(22, 6); readA(22, 18); compute(19); cs_step(20); LOAD_STAT(); issueB(0, 0, 14); SCHED();
  readB(23, 6); readA(23, 18); compute(20); cs_step(21); issueB(0, 1, 15); SCHED();
  bar<10>();
  readB(24, 10); readA(24, 8); compute(21); cs_step(22); issueB(1, 0, 6); SCHED();
  readB(25, 10); readA(25, 8); compute(22); cs_step(23); issueB(1, 1, 7); SCHED();
  readB(26, 10); readA(26, 8); compute(23); cs_step(24); issueB(2, 0, 16); SCHED();
  readB(27, 10); readA(27, 8); compute(24); cs_step(25); TOUCH_W2(); issueB(2, 1, 17); SCHED();
  bar<13>();
  readB(28, 1); compute(25); cs_step(26); issueB(3, 0, 8); SCHED();
  readB(29, 1); compute(26); cs_step(27); put_cs(true); issueB(3, 1, 9); SCHED();
  readB(30, 2); compute(27); cs_step(28); GET_STAT(9); issueB(4, 0, 10); SCHED();
  readB(31, 2); compute(28); cs_step(29); issueB(4, 1, 11); SCHED();
  bar<15>();
  CORRECT(acca[0], 0); CORRECT(acca[1], 1); readB(32, 3); compute(29); cs_step(30); issueB(5, 0, 18); SCHED();
  readB(33, 3); compute(30); cs_step(31); epa(0); epa(1); issueB(5, 1, 19); SCHED();
  readB(34, 12); compute(31); cs_step(32); epa(2); issueW(0, 1); SCHED();
  readB(35, 12); compute(32); cs_step(33); epa(3); SCHED();
  bar<16>();
  readB(36, 13); compute(33); cs_step(34); epa(4); epa(5); issueB(6, 0, 2); SCHED();
  readB(37, 13); compute(34); cs_step(35); epa(6); issueB(6, 1, 3); SCHED();
  readB(38, 4); compute(35); cs_step(36); epa(7); STORE_HA(0); SCHED();
  readB(39, 4); compute(36); cs_step(37); epa(8); epa(9); issueW(1, 12); SCHED();
  bar<19>();
  readB(40, 5); compute(37); cs_step(38); epa(10); issueW(2, 4); SCHED();
  readB(41, 5); compute(38); cs_step(39); epa(11); issueW(3, 13); SCHED();
  bar<18>();
  readB(42, 14); compute(39); cs_step(40); epa(12); epa(13); issueW(4, 5); SCHED();
  readB(43, 14); compute(40); cs_step(41); put_cs(false); epa(14); SCHED();
  readB(44, 14); compute(41); cs_step(42); epa(15); STORE_HA(1); SCHED();
  readB(45, 14); compute(42); cs_step(43); SCHED();
  bar<18>();
  CORRECT(accq, 0); readB(46, 6); compute(43); cs_step(44); issueW(5, 14); SCHED();
  readB(47, 6); compute(44); cs_step(45); qhalf(0); issueW(6, 15); SCHED();
  readB(48, 6); compute(45); cs_step(46); qhalf(1); STORE_Q(0); SCHED();
  readB(49, 6); compute(46); cs_step(47); qhalf(2); LOAD_XE(); SCHED();
  bar<7>();
  readB(50, 16); compute(47); cs_step(48); MAKE_TW(); qhalf(3); STORE_Q(1); SCHED();
  readB(51, 16); readW(0, 1); compute(48); cs_step(49); issueW(7, 6); SCHED();
  readB(52, 16); readW(1, 12); compute(49); cs_step(50); issueW(8, 7); SCHED();
  readB(53, 16); readW(2, 4); compute(50); cs_step(51); wvm<7>(); FLAG_A(); SCHED();
  bar<7>();
  readB(54, 8); compute(51); cs_step(52); FLAG_A2(); issueW(9, 1); SCHED();
  readB(55, 8); readW(3, 13); compute(52); cs_step(53); issueW(10, 4); SCHED();
  readB(56, 8); readW(4, 5); compute(53); cs_step(54); issueW(11, 12); SCHED();
  readB(57, 8); readW(5, 14); compute(54); cs_step(55); issueW(12, 16); SCHED();
  bar<5>();
  readB(58, 10); compute(55); cs_step(56); TOUCH_DONE(28); issueW(13, 5); SCHED();
  readB(59, 10); readW(6, 15); compute(56); cs_step(57); wvm<8>(); FLAG_Q(); issueW(14, 13); SCHED();
  readB(60, 10); readW(7, 6); compute(57); cs_step(58); POLL_A(); issueW(15, 14); SCHED();
  readB(61, 10); readW(8, 7); compute(58); cs_step(59); setup(12); issueW(16, 17); SCHED();
  bar<9>();
  readB(62, 18); compute(59); cs_step(60); setup(13); issueW(17, 15); SCHED();
  readB(63, 18); readW(9, 1); compute(60); cs_step(61); setup(14); issueW(18, 6); SCHED();
  readB(64, 18); compute(61); cs_step(62); issueW(19, 7); SCHED();
  readB(65, 18); compute(62); cs_step(63); CHECK_POLL(5); ldH(0); issueW(20, 8); SCHED();
  bar<63>();
  readB(66, 2); compute(63); cs_step(64); POLL_Q(); ldH(1); issueW(21, 1); SCHED();
  readB(67, 2); compute(64); cs_step(65); ldH(2); issueW(22, 9); SCHED();
  readB(68, 2); compute(65); cs_step(66); ldH(3); issueW(23, 10); SCHED();
  readB(69, 2); compute(66); cs_step(67); ldH(4); issueW(24, 11); SCHED();
  compute(67); cs_step(68); CHECK_Q(8); LOAD_ATT(0); LOAD_ATT(1); SCHED();
  compute(68); cs_step(69); put_cs(true); LOAD_ATT(2); LOAD_ATT(3); SCHED();
  bar<17>();
  LOAD_CS2(); compute(69); LOAD_ATT(4); ldH(5); issueW(25, 2); SCHED();
  CORRECT2();
  PRIO(2); STORE_HB(0); STORE_HB(1); PRIO(0);
  WAIT_ATT(4); wvm1<19>(Hf[0]); ldH(6); readW(10, 4); comp2(0); att(0); SCHED();
  wvm1<17>(Hf[1]); ldH(7); readW(11, 12); comp2(1); att(1); SCHED();
  wvm1<16>(Hf[2]); ldH(8); readW(12, 16); comp2(2); att(2); SCHED();
  wvm<3>(); FLAG_B();
  wvm1<16>(Hf[3]); ldH(9); readW(13, 5); comp2(3); att(3); SCHED();
  wvm1<15>(Hf[4]); ldH(10); readW(14, 13); comp2(4); att(4); SCHED();
  wvm1<9>(Hf[5]); ldH(11); readW(15, 14); comp2(5); att(5); SCHED();
  att(6); STORE_O(); wvm1<7>(Hf[6]); issueW(26, 3); readW(16, 17); comp2(6); SCHED();
  wvm1<7>(Hf[7]); issueW(27, 18); readW(17, 15); comp2(7); SCHED();
  bar<12>();
  wvm1<7>(Hf[8]); issueW(28, 0); issueW(29, 19); readW(18, 6); readW(19, 7); comp2(8); SCHED();
  wvm1<7>(Hf[9]); issueW(30, 4); issueW(31, 5); readW(20, 8); readW(21, 1); comp2(9); SCHED();
  wvm1<8>(Hf[10]); issueW(32, 12); issueW(33, 13); readW(22, 9); readW(23, 10); comp2(10); SCHED();
  POLL_B(); wvm1<10>(Hf[11]); issueW(34, 14); issueW(35, 15); readW(24, 11); readW(25, 2); comp2(11); SCHED();
  wvm<11>(); FLAG_O(); SPARE_EXIT();
  CHECK_POLL(3);
  ldH(12); ldH(13); ldH(14); ldH(15); ldH(16); ADDR_Y(); bar<6>();
  wvm1<4>(Hf[12]); ldH(17); readW(26, 3); comp2(12); SCHED();
  wvm1<4>(Hf[13]); ldH(18); readW(27, 18); comp2(13); SCHED();
  wvm1<4>(Hf[14]); ldH(19); readW(28, 0); comp2(14); SCHED();
  wvm1<4>(Hf[15]); ldH(20); readW(29, 19); comp2(15); SCHED();
  wvm1<4>(Hf[16]); ldH(21); readW(30, 4); comp2(16); SCHED();
  wvm1<4>(Hf[17]); ldH(22); readW(31, 5); comp2(17); SCHED();
  wvm1<4>(Hf[18]); ldH(23); POLL_O(); readW(32, 12); comp2(18); SCHED();
  wvm1<5>(Hf[19]); ldH(24); readW(33, 13); comp2(19); SCHED();
  wvm1<5>(Hf[20]); ldH(25); readW(34, 14); comp2(20); SCHED();
  wvm1<5>(Hf[21]); ldH(26); readW(35, 15); comp2(21); SCHED();
  wvm1<5>(Hf[22]); ldH(27); comp2(22); SCHED();
  wvm1<5>(Hf[23]); CHECK_POLL(4);
  ldH(28); comp2(23); SCHED();
  wvm1<4>(Hf[24]); ldH(29); comp2(24); SCHED();
  wvm1<4>(Hf[25]); ldH(30); comp2(25); SCHED();
  wvm1<4>(Hf[26]); ldH(31); comp2(26); SCHED();
  wvm1<4>(Hf[27]); ldH(32); comp2(27); SCHED();
  wvm1<4>(Hf[28]); ldH(33); comp2(28); PUT_Y(0); SCHED();
  wvm1<4>(Hf[29]); ldH(34); comp2(29); PUT_Y(1); SCHED();
  wvm1<4>(Hf[30]); ldH(35); comp2(30); PUT_Y(2); SCHED();
  wvm1<4>(Hf[31]); comp2(31); PUT_Y(3); SCHED();
  wvm1<3>(Hf[32]); comp2(32); PUT_Y(4); SCHED();
  wvm1<2>(Hf[33]); comp2(33); PUT_Y(5); SCHED();
  wvm1<1>(Hf[34]); comp2(34); SCHED();
  wvm1<0>(Hf[35]); comp2(35); SCHED();
  bar<63>();
  GET_Y0(); ADDR_S(); PUT_S(); GET_Y1(); wvm1<63>(xe);
  bar<63>();
  FINAL();
  asm volatile(".Lfk_end%c0:" ::"n"((int)qq));
  __builtin_amdgcn_endpgm();
}
#endif

__global__ __launch_bounds__(512, 2) void fused_kernel(const void* a0, const void* a1, const void* a2, const void* a3, const void* a4, const void* a5,
                                                       void* a6, void* a7) {
#if defined(__HIP_DEVICE_COMPILE__)
  __shared__ __attribute__((aligned(16))) char smem_[163840];
  const unsigned b = blockIdx.x;
  const unsigned c = 4 * (b & 3) + ((b >> 3) & 3);
  if (__builtin_expect(c >= 13, 0)) body<true>((LAS char*)smem_);
  else body<false>((LAS char*)smem_);
#endif
}

extern "C" void kernel_launch(void* const* d_in, const int* in_sizes, int n_in, void* d_out, int out_size, void* d_ws, size_t ws_size, hipStream_t stream) {
  fused_kernel<<<256, 512, 0, stream>>>(d_in[0], d_in[1], d_in[2], d_in[4], d_in[5], d_in[6], d_out, d_ws);
}
```
